```python
import torch

def _softmax(x, axis=-1):
    return torch.softmax(x, dim=axis)
def _gelu(x):
    return torch.nn.functional.gelu(x, approximate="tanh")
def _silu(x):
    return torch.nn.functional.silu(x)
def _sigmoid(x):
    return torch.sigmoid(x)
def _layernorm(x, axis=-1, eps=1e-5):
    m = x.mean(dim=axis, keepdim=True)
    v = x.var(dim=axis, keepdim=True, unbiased=False)
    return (x - m) / torch.sqrt(v + eps)
def _scatter_add(tgt, idx, src, axis=0):
    return tgt.clone().index_add_(axis, idx, src)
def _segment_sum(src, seg, n):
    out = torch.zeros((n,) + src.shape[1:], dtype=src.dtype)
    return out.index_add_(0, seg, src)
def _pad(x, pw):
    flat = tuple(v for pair in reversed(pw) for v in pair)
    return torch.nn.functional.pad(x, flat)

kernel_name = 'standalone__grn__sampling'

def setup_inputs():

    _w = (lambda a: (lambda t: t.to(torch.bfloat16) if t.is_floating_point() else t)(torch.as_tensor(a)))
    B, S, D = 1024, 8, 640
    return {
        "x": _w((lambda *s: torch.randn(*s, dtype=torch.bfloat16) * 0.05)(B, S, D)),
        "g": _w((lambda *s: torch.randn(*s, dtype=torch.bfloat16) * 0.4)(D) * 0.1),
        "b": _w((lambda *s: torch.randn(*s, dtype=torch.bfloat16) * 0.1)(D) * 0.1),
    }

def reference(x, g, b):
    gx = torch.sqrt((x * x).sum(axis=-2, keepdims=True))
    nx = gx / (gx.mean(axis=-1, keepdims=True) + 1e-6)
    return g * (x * nx) + b + x
```

```cpp
#include <hip/hip_runtime.h>
#include <hip/hip_bf16.h>
#include <stdint.h>

typedef uint32_t u32x4 __attribute__((ext_vector_type(4)));
typedef float f32x2 __attribute__((ext_vector_type(2)));
typedef __bf16 bf16x2 __attribute__((ext_vector_type(2)));

constexpr int S = 8, D = 640;

__device__ __forceinline__ float lo(uint32_t v) { return __uint_as_float(v << 16); }
__device__ __forceinline__ float hi(uint32_t v) { return __uint_as_float(v & 0xffff0000u); }
__device__ __forceinline__ uint32_t pk(float a, float b) {
    f32x2 f = {a, b};
    bf16x2 r = __builtin_convertvector(f, bf16x2);
    return __builtin_bit_cast(uint32_t, r);
}

__global__ __launch_bounds__(320) void grn_kernel(const uint16_t* __restrict__ x, const uint16_t* __restrict__ g,
                                                  const uint16_t* __restrict__ b, uint16_t* __restrict__ out) {
    __shared__ float part[20];
    const int tid = threadIdx.x;
    const int bl = tid / 80, c = tid - bl * 80;
    const size_t off = (size_t)(blockIdx.x * 4 + bl) * (S * D * 2) + c * 16;
    const char* xb = (const char*)x + off;
    char* ob = (char*)out + off;
    u32x4 a[8];
#pragma unroll
    for (int s = 0; s < 8; ++s) a[s] = *(const u32x4*)(xb + s * 1280);
    u32x4 ga = *(const u32x4*)((const char*)g + c * 16);
    u32x4 ba = *(const u32x4*)((const char*)b + c * 16);

    float sa[8];
#pragma unroll
    for (int j = 0; j < 8; ++j) sa[j] = 0.f;
#pragma unroll
    for (int s = 0; s < 8; ++s)
#pragma unroll
        for (int j = 0; j < 4; ++j) {
            float l = lo(a[s][j]), h = hi(a[s][j]);
            sa[2 * j] += l * l; sa[2 * j + 1] += h * h;
        }
    float p = 0.f;
#pragma unroll
    for (int j = 0; j < 8; ++j) { sa[j] = __builtin_amdgcn_sqrtf(sa[j]); p += sa[j]; }
#pragma unroll
    for (int o = 8; o > 0; o >>= 1) p += __shfl_xor(p, o, 64);
    if ((tid & 15) == 0) part[tid >> 4] = p;
    __syncthreads();
    float tot = part[bl * 5] + part[bl * 5 + 1] + part[bl * 5 + 2] + part[bl * 5 + 3] + part[bl * 5 + 4];
    float inv = 1.0f / (tot * (1.0f / D) + 1e-6f);
    float ca[8];
#pragma unroll
    for (int j = 0; j < 4; ++j) {
        ca[2 * j] = lo(ga[j]) * (sa[2 * j] * inv) + 1.0f;
        ca[2 * j + 1] = hi(ga[j]) * (sa[2 * j + 1] * inv) + 1.0f;
    }
#pragma unroll
    for (int s = 0; s < 8; ++s) {
        u32x4 o;
#pragma unroll
        for (int j = 0; j < 4; ++j)
            o[j] = pk(lo(a[s][j]) * ca[2 * j] + lo(ba[j]), hi(a[s][j]) * ca[2 * j + 1] + hi(ba[j]));
        *(u32x4*)(ob + s * 1280) = o;
    }
}

extern "C" void kernel_launch(void* const* d_in, const int* in_sizes, int n_in,
                              void* d_out, int out_size, void* d_ws, size_t ws_size,
                              hipStream_t stream) {
    const int B = in_sizes[0] / (S * D);
    grn_kernel<<<B / 4, 320, 0, stream>>>((const uint16_t*)d_in[0], (const uint16_t*)d_in[1],
                                          (const uint16_t*)d_in[2], (uint16_t*)d_out);
}
```

```cpp
#include <hip/hip_runtime.h>
#include <hip/hip_bf16.h>
#include <stdint.h>
#include <cstdio>

typedef uint32_t u32x4 __attribute__((ext_vector_type(4)));
typedef float f32x2 __attribute__((ext_vector_type(2)));
typedef __bf16 bf16x2 __attribute__((ext_vector_type(2)));

constexpr int S = 8, D = 640;

__device__ __forceinline__ float lo(uint32_t v) { return __uint_as_float(v << 16); }
__device__ __forceinline__ float hi(uint32_t v) { return __uint_as_float(v & 0xffff0000u); }
__device__ __forceinline__ uint32_t pk(float a, float b) {
    f32x2 f = {a, b};
    bf16x2 r = __builtin_convertvector(f, bf16x2);
    return __builtin_bit_cast(uint32_t, r);
}

__global__ __launch_bounds__(320) void grn_kernel(const uint16_t* __restrict__ x, const uint16_t* __restrict__ g,
                                                  const uint16_t* __restrict__ b, uint16_t* __restrict__ out, unsigned long long* ws) {
    unsigned long long r0=__builtin_amdgcn_s_memrealtime(), c0=__builtin_amdgcn_s_memtime();
    __shared__ float part[20];
    const int tid = threadIdx.x;
    const int bl = tid / 80, c = tid - bl * 80;
    const size_t off = (size_t)(blockIdx.x * 4 + bl) * (S * D * 2) + c * 16;
    const char* xb = (const char*)x + off;
    char* ob = (char*)out + off;
    u32x4 a[8];
#pragma unroll
    for (int s = 0; s < 8; ++s) a[s] = *(const u32x4*)(xb + s * 1280);
    u32x4 ga = *(const u32x4*)((const char*)g + c * 16);
    u32x4 ba = *(const u32x4*)((const char*)b + c * 16);

    float sa[8];
#pragma unroll
    for (int j = 0; j < 8; ++j) sa[j] = 0.f;
#pragma unroll
    for (int s = 0; s < 8; ++s)
#pragma unroll
        for (int j = 0; j < 4; ++j) {
            float l = lo(a[s][j]), h = hi(a[s][j]);
            sa[2 * j] += l * l; sa[2 * j + 1] += h * h;
        }
    asm volatile("" :: "v"(sa[0]), "v"(sa[7]));
    unsigned long long r1=__builtin_amdgcn_s_memrealtime();
    float p = 0.f;
#pragma unroll
    for (int j = 0; j < 8; ++j) { sa[j] = __builtin_amdgcn_sqrtf(sa[j]); p += sa[j]; }
#pragma unroll
    for (int o = 8; o > 0; o >>= 1) p += __shfl_xor(p, o, 64);
    if ((tid & 15) == 0) part[tid >> 4] = p;
    __syncthreads();
    float tot = part[bl * 5] + part[bl * 5 + 1] + part[bl * 5 + 2] + part[bl * 5 + 3] + part[bl * 5 + 4];
    float inv = 1.0f / (tot * (1.0f / D) + 1e-6f);
    float ca[8];
#pragma unroll
    for (int j = 0; j < 4; ++j) {
        ca[2 * j] = lo(ga[j]) * (sa[2 * j] * inv) + 1.0f;
        ca[2 * j + 1] = hi(ga[j]) * (sa[2 * j + 1] * inv) + 1.0f;
    }
#pragma unroll
    for (int s = 0; s < 8; ++s) {
        u32x4 o;
#pragma unroll
        for (int j = 0; j < 4; ++j)
            o[j] = pk(lo(a[s][j]) * ca[2 * j] + lo(ba[j]), hi(a[s][j]) * ca[2 * j + 1] + hi(ba[j]));
        *(u32x4*)(ob + s * 1280) = o;
    }
    unsigned long long r2=__builtin_amdgcn_s_memrealtime();
    asm volatile("s_waitcnt vmcnt(0)" ::: "memory");
    unsigned long long r3=__builtin_amdgcn_s_memrealtime(), c3=__builtin_amdgcn_s_memtime();
    if (tid == 0) { ws[blockIdx.x*8+0]=r0; ws[blockIdx.x*8+1]=r1; ws[blockIdx.x*8+2]=r2; ws[blockIdx.x*8+3]=r3; ws[blockIdx.x*8+4]=c3-c0; }
}
__global__ void report(unsigned long long* ws) {
    unsigned long long mn=~0ull, mx0=0, mx1=0, mn1=~0ull, mx2=0, mx3=0, mn3=~0ull;
    for (int i=0;i<256;++i){ unsigned long long a=ws[i*8],b=ws[i*8+1],c=ws[i*8+2],d=ws[i*8+3];
      mn=min(mn,a); mx0=max(mx0,a); mn1=min(mn1,b); mx1=max(mx1,b); mx2=max(mx2,c); mx3=max(mx3,d); mn3=min(mn3,d);}
    printf("dev: laststart %llu firstload %llu lastload %llu laststoreissue %llu firstend %llu lastend %llu (10ns ticks); blk0 cyc %llu rt %llu\n", mx0-mn, mn1-mn, mx1-mn, mx2-mn, mn3-mn, mx3-mn, ws[4], ws[3]-ws[0]);
}

extern "C" void kernel_launch(void* const* d_in, const int* in_sizes, int n_in,
                              void* d_out, int out_size, void* d_ws, size_t ws_size,
                              hipStream_t stream) {
    const int B = in_sizes[0] / (S * D);
    grn_kernel<<<B / 4, 320, 0, stream>>>((const uint16_t*)d_in[0], (const uint16_t*)d_in[1],
                                          (const uint16_t*)d_in[2], (uint16_t*)d_out, (unsigned long long*)d_ws);
    report<<<1,1,0,stream>>>((unsigned long long*)d_ws);
    void* ps[5]={d_in[0],d_in[1],d_in[2],d_out,d_ws};
    for (int i=0;i<5;++i){ hipPointerAttribute_t at; hipError_t e=hipPointerGetAttributes(&at, ps[i]);
      void* base=0; size_t sz=0; hipMemGetAddressRange((hipDeviceptr_t*)&base,&sz,(hipDeviceptr_t)ps[i]);
      fprintf(stderr,"ptr %d %p err %d type %d dev %d managed %d flags %u base %p size %zu\n", i, ps[i], (int)e, (int)at.type, at.device, at.isManaged, at.allocationFlags, base, sz);}
}
```

```cpp
#define METRIC 0
#include <hip/hip_runtime.h>
#include <hip/hip_bf16.h>
#include <stdint.h>
#include <cstdio>

typedef uint32_t u32x4 __attribute__((ext_vector_type(4)));
typedef float f32x2 __attribute__((ext_vector_type(2)));
typedef __bf16 bf16x2 __attribute__((ext_vector_type(2)));

constexpr int S = 8, D = 640;

__device__ __forceinline__ float lo(uint32_t v) { return __uint_as_float(v << 16); }
__device__ __forceinline__ float hi(uint32_t v) { return __uint_as_float(v & 0xffff0000u); }
__device__ __forceinline__ uint32_t pk(float a, float b) {
    f32x2 f = {a, b};
    bf16x2 r = __builtin_convertvector(f, bf16x2);
    return __builtin_bit_cast(uint32_t, r);
}

__global__ __launch_bounds__(320) void grn_kernel(const uint16_t* __restrict__ x, const uint16_t* __restrict__ g,
                                                  const uint16_t* __restrict__ b, uint16_t* __restrict__ out, unsigned long long* ws) {
    unsigned long long r0=__builtin_amdgcn_s_memrealtime(), c0=__builtin_amdgcn_s_memtime();
    __shared__ float part[20];
    const int tid = threadIdx.x;
    const int bl = tid / 80, c = tid - bl * 80;
    const size_t off = (size_t)(blockIdx.x * 4 + bl) * (S * D * 2) + c * 16;
    const char* xb = (const char*)x + off;
    char* ob = (char*)out + off;
    u32x4 a[8];
#pragma unroll
    for (int s = 0; s < 8; ++s) a[s] = *(const u32x4*)(xb + s * 1280);
    u32x4 ga = *(const u32x4*)((const char*)g + c * 16);
    u32x4 ba = *(const u32x4*)((const char*)b + c * 16);

    float sa[8];
#pragma unroll
    for (int j = 0; j < 8; ++j) sa[j] = 0.f;
#pragma unroll
    for (int s = 0; s < 8; ++s)
#pragma unroll
        for (int j = 0; j < 4; ++j) {
            float l = lo(a[s][j]), h = hi(a[s][j]);
            sa[2 * j] += l * l; sa[2 * j + 1] += h * h;
        }
    asm volatile("" :: "v"(sa[0]), "v"(sa[7]));
    unsigned long long r1=__builtin_amdgcn_s_memrealtime();
    float p = 0.f;
#pragma unroll
    for (int j = 0; j < 8; ++j) { sa[j] = __builtin_amdgcn_sqrtf(sa[j]); p += sa[j]; }
#pragma unroll
    for (int o = 8; o > 0; o >>= 1) p += __shfl_xor(p, o, 64);
    if ((tid & 15) == 0) part[tid >> 4] = p;
    __syncthreads();
    float tot = part[bl * 5] + part[bl * 5 + 1] + part[bl * 5 + 2] + part[bl * 5 + 3] + part[bl * 5 + 4];
    float inv = 1.0f / (tot * (1.0f / D) + 1e-6f);
    float ca[8];
#pragma unroll
    for (int j = 0; j < 4; ++j) {
        ca[2 * j] = lo(ga[j]) * (sa[2 * j] * inv) + 1.0f;
        ca[2 * j + 1] = hi(ga[j]) * (sa[2 * j + 1] * inv) + 1.0f;
    }
#pragma unroll
    for (int s = 0; s < 8; ++s) {
        u32x4 o;
#pragma unroll
        for (int j = 0; j < 4; ++j)
            o[j] = pk(lo(a[s][j]) * ca[2 * j] + lo(ba[j]), hi(a[s][j]) * ca[2 * j + 1] + hi(ba[j]));
        *(u32x4*)(ob + s * 1280) = o;
    }
    unsigned long long r2=__builtin_amdgcn_s_memrealtime();
    asm volatile("s_waitcnt vmcnt(0)" ::: "memory");
    unsigned long long r3=__builtin_amdgcn_s_memrealtime(), c3=__builtin_amdgcn_s_memtime();
    if (tid == 0) { ws[blockIdx.x*8+0]=r0; ws[blockIdx.x*8+1]=r1; ws[blockIdx.x*8+2]=r2; ws[blockIdx.x*8+3]=r3; ws[blockIdx.x*8+4]=c3-c0; }
}
__global__ void report(unsigned long long* ws) {
    int i = threadIdx.x;
    unsigned long long a=ws[i*8],b=ws[i*8+1],c=ws[i*8+2],d=ws[i*8+3], cy=ws[i*8+4];
    __shared__ unsigned long long sm[8];
    if (i==0){ sm[0]=~0ull; sm[1]=0; sm[2]=~0ull; sm[3]=0; sm[4]=0; sm[5]=~0ull; sm[6]=0; }
    __syncthreads();
    atomicMin(&sm[0],a); atomicMax(&sm[1],a); atomicMin(&sm[2],b); atomicMax(&sm[3],b); atomicMax(&sm[4],c); atomicMin(&sm[5],d); atomicMax(&sm[6],d);
    __syncthreads();
    if (i==0) {
      unsigned long long now=__builtin_amdgcn_s_memrealtime();
      long long v;
      if (METRIC==0) v=0;
      else if (METRIC==7) v = (long long)(cy*10/(d-a));
      else if (METRIC==8) v = (long long)(now - sm[6]);
      else v=(long long)(sm[METRIC]-sm[0]);
      if (v<0) v=0; if (v>3000) v=3000;
      unsigned long long t0=__builtin_amdgcn_s_memrealtime();
      while (__builtin_amdgcn_s_memrealtime()-t0 < (unsigned long long)v*100) __builtin_amdgcn_s_sleep(1);
    }
}

extern "C" void kernel_launch(void* const* d_in, const int* in_sizes, int n_in,
                              void* d_out, int out_size, void* d_ws, size_t ws_size,
                              hipStream_t stream) {
    const int B = in_sizes[0] / (S * D);
    grn_kernel<<<B / 4, 320, 0, stream>>>((const uint16_t*)d_in[0], (const uint16_t*)d_in[1],
                                          (const uint16_t*)d_in[2], (uint16_t*)d_out, (unsigned long long*)d_ws);
    report<<<1,256,0,stream>>>((unsigned long long*)d_ws);

}
```

```cpp
#include <hip/hip_runtime.h>
#include <hip/hip_bf16.h>
#include <stdint.h>

typedef uint32_t u32x4 __attribute__((ext_vector_type(4)));
typedef float f32x2 __attribute__((ext_vector_type(2)));
typedef __bf16 bf16x2 __attribute__((ext_vector_type(2)));

constexpr int S = 8, D = 640;

__device__ __forceinline__ float lo(uint32_t v) { return __uint_as_float(v << 16); }
__device__ __forceinline__ float hi(uint32_t v) { return __uint_as_float(v & 0xffff0000u); }
__device__ __forceinline__ uint32_t pk(float a, float b) {
    f32x2 f = {a, b};
    bf16x2 r = __builtin_convertvector(f, bf16x2);
    return __builtin_bit_cast(uint32_t, r);
}

__global__ __launch_bounds__(320) void grn_kernel(const uint16_t* __restrict__ x, const uint16_t* __restrict__ g,
                                                  const uint16_t* __restrict__ b, uint16_t* __restrict__ out) {
    __shared__ float part[20];
    const int tid = threadIdx.x;
    const int bl = tid / 80, c = tid - bl * 80;
    const size_t off = (size_t)(blockIdx.x * 4 + bl) * (S * D * 2) + c * 16;
    const char* xb = (const char*)x + off;
    char* ob = (char*)out + off;
    u32x4 a[8];
#pragma unroll
    for (int s = 0; s < 8; ++s) a[s] = *(const u32x4*)(xb + s * 1280);
    u32x4 ga = *(const u32x4*)((const char*)g + c * 16);
    u32x4 ba = *(const u32x4*)((const char*)b + c * 16);

    float sa[8];
#pragma unroll
    for (int j = 0; j < 8; ++j) sa[j] = 0.f;
#pragma unroll
    for (int s = 0; s < 8; ++s)
#pragma unroll
        for (int j = 0; j < 4; ++j) {
            float l = lo(a[s][j]), h = hi(a[s][j]);
            sa[2 * j] += l * l; sa[2 * j + 1] += h * h;
        }
    float p = 0.f;
#pragma unroll
    for (int j = 0; j < 8; ++j) { sa[j] = __builtin_amdgcn_sqrtf(sa[j]); p += sa[j]; }
#pragma unroll
    for (int o = 8; o > 0; o >>= 1) p += __shfl_xor(p, o, 64);
    if ((tid & 15) == 0) part[tid >> 4] = p;
    __syncthreads();
    float tot = part[bl * 5] + part[bl * 5 + 1] + part[bl * 5 + 2] + part[bl * 5 + 3] + part[bl * 5 + 4];
    float inv = 1.0f / (tot * (1.0f / D) + 1e-6f);
    float ca[8];
#pragma unroll
    for (int j = 0; j < 4; ++j) {
        ca[2 * j] = lo(ga[j]) * (sa[2 * j] * inv) + 1.0f;
        ca[2 * j + 1] = hi(ga[j]) * (sa[2 * j + 1] * inv) + 1.0f;
    }
#pragma unroll
    for (int s = 0; s < 8; ++s) {
        u32x4 o;
#pragma unroll
        for (int j = 0; j < 4; ++j)
            o[j] = pk(lo(a[s][j]) * ca[2 * j] + lo(ba[j]), hi(a[s][j]) * ca[2 * j + 1] + hi(ba[j]));
        asm volatile("global_store_dwordx4 %0, %1, off sc0 sc1\n\ts_nop 1" :: "v"(ob + s * 1280), "v"(o) : "memory");
    }
}

extern "C" void kernel_launch(void* const* d_in, const int* in_sizes, int n_in,
                              void* d_out, int out_size, void* d_ws, size_t ws_size,
                              hipStream_t stream) {
    const int B = in_sizes[0] / (S * D);
    grn_kernel<<<B / 4, 320, 0, stream>>>((const uint16_t*)d_in[0], (const uint16_t*)d_in[1],
                                          (const uint16_t*)d_in[2], (uint16_t*)d_out);
}
```

```cpp
#include <hip/hip_runtime.h>
#include <hip/hip_bf16.h>
#include <stdint.h>

typedef uint32_t u32x4 __attribute__((ext_vector_type(4)));
typedef float f32x2 __attribute__((ext_vector_type(2)));
typedef __bf16 bf16x2 __attribute__((ext_vector_type(2)));

constexpr int B = 1024, S = 8, D = 640;
constexpr int WPB = 4;
constexpr int NBLK = B / WPB;
constexpr int ROW = D * 2, BATCH = S * ROW;
constexpr int SPLIT = 4;
constexpr int LD_AUX = 2;
constexpr int ST_AUX = 19;

__device__ __forceinline__ f32x2 up(uint32_t v) {
    f32x2 r = {__uint_as_float(v << 16), __uint_as_float(v & 0xffff0000u)};
    return r;
}
__device__ __forceinline__ uint32_t pk(f32x2 f) {
    return __builtin_bit_cast(uint32_t, __builtin_convertvector(f, bf16x2));
}
__device__ __forceinline__ f32x2 fma2(f32x2 a, f32x2 b, f32x2 c) { return __builtin_elementwise_fma(a, b, c); }
template <int CTRL>
__device__ __forceinline__ float dpp_add(float v) {
    return v + __int_as_float(__builtin_amdgcn_update_dpp(0, __float_as_int(v), CTRL, 0xf, 0xf, true));
}
__device__ __forceinline__ void swap32(float& a, float& b) {
    auto r = __builtin_amdgcn_permlane32_swap(__float_as_uint(a), __float_as_uint(b), false, false);
    a = __uint_as_float(r[0]); b = __uint_as_float(r[1]);
}
__device__ __forceinline__ void swap16(float& a, float& b) {
    auto r = __builtin_amdgcn_permlane16_swap(__float_as_uint(a), __float_as_uint(b), false, false);
    a = __uint_as_float(r[0]); b = __uint_as_float(r[1]);
}
#define SB() __builtin_amdgcn_sched_barrier(0)

__global__ __launch_bounds__(64 * WPB) void grn_kernel(const void* __restrict__ x, const void* __restrict__ g,
                                                       const void* __restrict__ b, void* __restrict__ out) {
    SB();
    const int lane = threadIdx.x & 63;
    const int wv = __builtin_amdgcn_readfirstlane(threadIdx.x >> 6);
    const int batch = (blockIdx.x & 7) * (NBLK / 8) + (blockIdx.x >> 3) + wv * NBLK;
    const int so = batch * BATCH;
    const int vm = lane * 16;
    const int vc = 1024 + (lane & 15) * 16;
    int vt = (lane >> 4) * ROW + vc, so4 = so + 4 * ROW;
    asm volatile("" : "+v"(vt), "+s"(so4) : "v"(vm), "s"(so));
    SB();
    const __amdgpu_buffer_rsrc_t rx = __builtin_amdgcn_make_buffer_rsrc((void*)x, 0, B * BATCH, 0x00020000);
    const __amdgpu_buffer_rsrc_t ro = __builtin_amdgcn_make_buffer_rsrc(out, 0, B * BATCH, 0x00020000);
    const __amdgpu_buffer_rsrc_t rg = __builtin_amdgcn_make_buffer_rsrc((void*)g, 0, ROW, 0x00020000);
    const __amdgpu_buffer_rsrc_t rb = __builtin_amdgcn_make_buffer_rsrc((void*)b, 0, ROW, 0x00020000);
    SB();
    u32x4 t[2], a[8], gt, ga, bt, ba;
    t[0] = __builtin_amdgcn_raw_buffer_load_b128(rx, vt, so, LD_AUX);
    t[1] = __builtin_amdgcn_raw_buffer_load_b128(rx, vt, so4, LD_AUX);
#pragma unroll
    for (int s = 0; s < 8; ++s) {
        a[s] = __builtin_amdgcn_raw_buffer_load_b128(rx, vm + (s & 3) * ROW, s < 4 ? so : so4, LD_AUX);
        SB();
        if (s == SPLIT) {
            asm volatile("v_nop");
            SB();
            gt = __builtin_amdgcn_raw_buffer_load_b128(rg, vc, 0, 0);
            ga = __builtin_amdgcn_raw_buffer_load_b128(rg, vm, 0, 0);
            bt = __builtin_amdgcn_raw_buffer_load_b128(rb, vc, 0, 0);
            ba = __builtin_amdgcn_raw_buffer_load_b128(rb, vm, 0, 0);
            SB();
        }
    }

    f32x2 xt[2][4], st[4];
#pragma unroll
    for (int j = 0; j < 4; ++j) { xt[0][j] = up(t[0][j]); st[j] = xt[0][j] * xt[0][j]; }
#pragma unroll
    for (int j = 0; j < 4; ++j) { xt[1][j] = up(t[1][j]); st[j] = fma2(xt[1][j], xt[1][j], st[j]); }
    float u0 = st[0][0], u1 = st[0][1], u2 = st[1][0], u3 = st[1][1];
    float u4 = st[2][0], u5 = st[2][1], u6 = st[3][0], u7 = st[3][1];
    swap32(u0, u4); swap32(u1, u5); swap32(u2, u6); swap32(u3, u7);
    u0 += u4; u1 += u5; u2 += u6; u3 += u7;
    swap16(u0, u2); swap16(u1, u3);
    u0 += u2; u1 += u3;
    u0 = __builtin_amdgcn_sqrtf(u0); u1 = __builtin_amdgcn_sqrtf(u1);
    float p = u0 + u1;
    u2 = u0; u3 = u1;
    swap16(u0, u2); swap16(u1, u3);
    u4 = u0; u5 = u1; u6 = u2; u7 = u3;
    swap32(u0, u4); swap32(u1, u5); swap32(u2, u6); swap32(u3, u7);
    f32x2 nt[4] = {{u0, u1}, {u2, u3}, {u4, u5}, {u6, u7}};
    SB();

    f32x2 xa[8][4], sa[4], fg[4], fa[4], ft[4];
#pragma unroll
    for (int s = 0; s < 8; ++s) {
#pragma unroll
        for (int j = 0; j < 4; ++j) {
            xa[s][j] = up(a[s][j]);
            sa[j] = s == 0 ? xa[s][j] * xa[s][j] : fma2(xa[s][j], xa[s][j], sa[j]);
        }
        asm volatile("" : "+v"(sa[0]), "+v"(sa[1]), "+v"(sa[2]), "+v"(sa[3]));
        if (s == SPLIT) {
#pragma unroll
            for (int j = 0; j < 4; ++j) { fg[j] = up(ga[j]); nt[j] *= up(gt[j]); fa[j] = up(ba[j]); ft[j] = up(bt[j]); }
            asm volatile("" : "+v"(fg[0]), "+v"(fg[1]), "+v"(fg[2]), "+v"(fg[3]), "+v"(nt[0]), "+v"(nt[1]), "+v"(nt[2]), "+v"(nt[3]));
            asm volatile("" : "+v"(fa[0]), "+v"(fa[1]), "+v"(fa[2]), "+v"(fa[3]), "+v"(ft[0]), "+v"(ft[1]), "+v"(ft[2]), "+v"(ft[3]));
        }
        SB();
    }
#pragma unroll
    for (int j = 0; j < 4; ++j) {
        sa[j][0] = __builtin_amdgcn_sqrtf(sa[j][0]);
        sa[j][1] = __builtin_amdgcn_sqrtf(sa[j][1]);
    }
    f32x2 q = (sa[0] + sa[1]) + (sa[2] + sa[3]);
    p += q[0] + q[1];
    p = dpp_add<0xB1>(p);
    p = dpp_add<0x4E>(p);
    p = dpp_add<0x141>(p);
    p = dpp_add<0x140>(p);
    { float r = p; swap16(p, r); p += r; r = p; swap32(p, r); p += r; }
    const float inv = __builtin_amdgcn_rcpf(p * (1.0f / D) + 1e-6f);
    const f32x2 inv2 = {inv, inv}, one = {1.0f, 1.0f};
    f32x2 ca[4], ct[4];
#pragma unroll
    for (int j = 0; j < 4; ++j) {
        ca[j] = fma2(fg[j] * sa[j], inv2, one);
        ct[j] = fma2(nt[j], inv2, one);
    }
    SB();
    u32x4 o[10];
#pragma unroll
    for (int s = 0; s < 10; ++s) {
#pragma unroll
        for (int j = 0; j < 4; ++j)
            o[s][j] = s < 8 ? pk(fma2(xa[s & 7][j], ca[j], fa[j])) : pk(fma2(xt[s & 1][j], ct[j], ft[j]));
        if (s > 0) asm volatile("" :: "v"(o[s - 1]), "v"(o[s]));
        if (s < 8) __builtin_amdgcn_raw_buffer_store_b128(o[s], ro, vm + (s & 3) * ROW, s < 4 ? so : so4, ST_AUX);
        else __builtin_amdgcn_raw_buffer_store_b128(o[s], ro, vt, (s & 1) ? so4 : so, ST_AUX);
        SB();
    }
}

extern "C" void kernel_launch(void* const* d_in, const int* in_sizes, int n_in,
                              void* d_out, int out_size, void* d_ws, size_t ws_size,
                              hipStream_t stream) {
    grn_kernel<<<NBLK, 64 * WPB, 0, stream>>>(d_in[0], d_in[1], d_in[2], d_out);
}
```

```cpp
#include <hip/hip_runtime.h>
#include <hip/hip_bf16.h>
#include <stdint.h>

typedef uint32_t u32x4 __attribute__((ext_vector_type(4)));
typedef float f32x2 __attribute__((ext_vector_type(2)));
typedef __bf16 bf16x2 __attribute__((ext_vector_type(2)));

constexpr int B = 1024, S = 8, D = 640;
constexpr int WPB = 4;
constexpr int NBLK = B / WPB;
constexpr int ROW = D * 2, BATCH = S * ROW;
constexpr int SPLIT = 4;
constexpr int LD_AUX = 2;
constexpr int ST_AUX = 19;

__device__ __forceinline__ f32x2 up(uint32_t v) {
    f32x2 r = {__uint_as_float(v << 16), __uint_as_float(v & 0xffff0000u)};
    return r;
}
__device__ __forceinline__ uint32_t pk(f32x2 f) {
    return __builtin_bit_cast(uint32_t, __builtin_convertvector(f, bf16x2));
}
__device__ __forceinline__ f32x2 fma2(f32x2 a, f32x2 b, f32x2 c) { return __builtin_elementwise_fma(a, b, c); }
template <int CTRL>
__device__ __forceinline__ float dpp_add(float v) {
    return v + __int_as_float(__builtin_amdgcn_update_dpp(0, __float_as_int(v), CTRL, 0xf, 0xf, true));
}
__device__ __forceinline__ void swap32(float& a, float& b) {
    auto r = __builtin_amdgcn_permlane32_swap(__float_as_uint(a), __float_as_uint(b), false, false);
    a = __uint_as_float(r[0]); b = __uint_as_float(r[1]);
}
__device__ __forceinline__ void swap16(float& a, float& b) {
    auto r = __builtin_amdgcn_permlane16_swap(__float_as_uint(a), __float_as_uint(b), false, false);
    a = __uint_as_float(r[0]); b = __uint_as_float(r[1]);
}
#define SB() __builtin_amdgcn_sched_barrier(0)

__global__ __launch_bounds__(64 * WPB) void grn_kernel(const void* __restrict__ x, const void* __restrict__ g,
                                                       const void* __restrict__ b, void* __restrict__ out) {
    __shared__ u32x4 gb[WPB * 64];
    SB();
    const int lane = threadIdx.x & 63;
    const int wv = __builtin_amdgcn_readfirstlane(threadIdx.x >> 6);
    const int batch = (blockIdx.x & 7) * (NBLK / 8) + (blockIdx.x >> 3) + wv * NBLK;
    const int so = batch * BATCH;
    const int vm = lane * 16;
    int vt = (lane >> 4) * ROW + 1024 + (lane & 15) * 16, so4 = so + 4 * ROW, vp = vm + (wv >> 1) * 1024;
    asm volatile("" : "+v"(vt), "+s"(so4), "+v"(vp) : "v"(vm), "s"(so));
    SB();
    const __amdgpu_buffer_rsrc_t rx = __builtin_amdgcn_make_buffer_rsrc((void*)x, 0, B * BATCH, 0x00020000);
    const __amdgpu_buffer_rsrc_t ro = __builtin_amdgcn_make_buffer_rsrc(out, 0, B * BATCH, 0x00020000);
    SB();
    u32x4 t[2], a[8];
    t[0] = __builtin_amdgcn_raw_buffer_load_b128(rx, vt, so, LD_AUX);
    t[1] = __builtin_amdgcn_raw_buffer_load_b128(rx, vt, so4, LD_AUX);
    SB();
    const __amdgpu_buffer_rsrc_t rp = __builtin_amdgcn_make_buffer_rsrc((void*)((wv & 1) ? b : g), 0, ROW, 0x00020000);
    const u32x4 pc = __builtin_amdgcn_raw_buffer_load_b128(rp, vp, 0, 0);
    SB();
#pragma unroll
    for (int s = 0; s < 8; ++s) {
        a[s] = __builtin_amdgcn_raw_buffer_load_b128(rx, vm + (s & 3) * ROW, s < 4 ? so : so4, LD_AUX);
        SB();
        if (s == SPLIT) {
            asm volatile("v_nop");
            SB();
        }
    }

    f32x2 xt[2][4], st[4];
#pragma unroll
    for (int j = 0; j < 4; ++j) { xt[0][j] = up(t[0][j]); st[j] = xt[0][j] * xt[0][j]; }
#pragma unroll
    for (int j = 0; j < 4; ++j) { xt[1][j] = up(t[1][j]); st[j] = fma2(xt[1][j], xt[1][j], st[j]); }
    float u0 = st[0][0], u1 = st[0][1], u2 = st[1][0], u3 = st[1][1];
    float u4 = st[2][0], u5 = st[2][1], u6 = st[3][0], u7 = st[3][1];
    swap32(u0, u4); swap32(u1, u5); swap32(u2, u6); swap32(u3, u7);
    u0 += u4; u1 += u5; u2 += u6; u3 += u7;
    swap16(u0, u2); swap16(u1, u3);
    u0 += u2; u1 += u3;
    u0 = __builtin_amdgcn_sqrtf(u0); u1 = __builtin_amdgcn_sqrtf(u1);
    float p = u0 + u1;
    u2 = u0; u3 = u1;
    swap16(u0, u2); swap16(u1, u3);
    u4 = u0; u5 = u1; u6 = u2; u7 = u3;
    swap32(u0, u4); swap32(u1, u5); swap32(u2, u6); swap32(u3, u7);
    f32x2 nt[4] = {{u0, u1}, {u2, u3}, {u4, u5}, {u6, u7}};
    SB();
    gb[threadIdx.x] = pc;
    asm volatile("s_waitcnt lgkmcnt(0)\n\ts_barrier" ::: "memory");
    const u32x4 ga = gb[lane], ba = gb[64 + lane], gt = gb[128 + (lane & 15)], bt = gb[192 + (lane & 15)];
    SB();

    f32x2 xa[8][4], sa[4], fg[4], fa[4], ft[4];
#pragma unroll
    for (int s = 0; s < 8; ++s) {
#pragma unroll
        for (int j = 0; j < 4; ++j) {
            xa[s][j] = up(a[s][j]);
            sa[j] = s == 0 ? xa[s][j] * xa[s][j] : fma2(xa[s][j], xa[s][j], sa[j]);
        }
        asm volatile("" : "+v"(sa[0]), "+v"(sa[1]), "+v"(sa[2]), "+v"(sa[3]));
        if (s == SPLIT) {
#pragma unroll
            for (int j = 0; j < 4; ++j) { fg[j] = up(ga[j]); nt[j] *= up(gt[j]); fa[j] = up(ba[j]); ft[j] = up(bt[j]); }
            asm volatile("" : "+v"(fg[0]), "+v"(fg[1]), "+v"(fg[2]), "+v"(fg[3]), "+v"(nt[0]), "+v"(nt[1]), "+v"(nt[2]), "+v"(nt[3]));
            asm volatile("" : "+v"(fa[0]), "+v"(fa[1]), "+v"(fa[2]), "+v"(fa[3]), "+v"(ft[0]), "+v"(ft[1]), "+v"(ft[2]), "+v"(ft[3]));
        }
        SB();
    }
#pragma unroll
    for (int j = 0; j < 4; ++j) {
        sa[j][0] = __builtin_amdgcn_sqrtf(sa[j][0]);
        sa[j][1] = __builtin_amdgcn_sqrtf(sa[j][1]);
    }
    f32x2 q = (sa[0] + sa[1]) + (sa[2] + sa[3]);
    p += q[0] + q[1];
    p = dpp_add<0xB1>(p);
    p = dpp_add<0x4E>(p);
    p = dpp_add<0x141>(p);
    p = dpp_add<0x140>(p);
    { float r = p; swap16(p, r); p += r; r = p; swap32(p, r); p += r; }
    const float inv = __builtin_amdgcn_rcpf(p * (1.0f / D) + 1e-6f);
    const f32x2 inv2 = {inv, inv}, one = {1.0f, 1.0f};
    f32x2 ca[4], ct[4];
#pragma unroll
    for (int j = 0; j < 4; ++j) {
        ca[j] = fma2(fg[j] * sa[j], inv2, one);
        ct[j] = fma2(nt[j], inv2, one);
    }
    SB();
    u32x4 o[10];
#pragma unroll
    for (int s = 0; s < 10; ++s) {
#pragma unroll
        for (int j = 0; j < 4; ++j)
            o[s][j] = s < 8 ? pk(fma2(xa[s & 7][j], ca[j], fa[j])) : pk(fma2(xt[s & 1][j], ct[j], ft[j]));
        if (s > 0) asm volatile("" :: "v"(o[s - 1]), "v"(o[s]));
        if (s < 8) __builtin_amdgcn_raw_buffer_store_b128(o[s], ro, vm + (s & 3) * ROW, s < 4 ? so : so4, ST_AUX);
        else __builtin_amdgcn_raw_buffer_store_b128(o[s], ro, vt, (s & 1) ? so4 : so, ST_AUX);
        SB();
    }
}

extern "C" void kernel_launch(void* const* d_in, const int* in_sizes, int n_in,
                              void* d_out, int out_size, void* d_ws, size_t ws_size,
                              hipStream_t stream) {
    grn_kernel<<<NBLK, 64 * WPB, 0, stream>>>(d_in[0], d_in[1], d_in[2], d_out);
}
```

```cpp
#include <hip/hip_runtime.h>
#include <hip/hip_bf16.h>
#include <stdint.h>

typedef uint32_t u32x4 __attribute__((ext_vector_type(4)));
typedef float f32x2 __attribute__((ext_vector_type(2)));
typedef __bf16 bf16x2 __attribute__((ext_vector_type(2)));

constexpr int B = 1024, S = 8, D = 640;
constexpr int WPB = 4;
constexpr int NBLK = B / WPB;
constexpr int ROW = D * 2, BATCH = S * ROW;
constexpr int SPLIT = 4;
constexpr int LD_AUX = 2;
constexpr int ST_AUX = 19;

__device__ __forceinline__ f32x2 up(uint32_t v) {
    f32x2 r = {__uint_as_float(v << 16), __uint_as_float(v & 0xffff0000u)};
    return r;
}
__device__ __forceinline__ uint32_t pk(f32x2 f) {
    return __builtin_bit_cast(uint32_t, __builtin_convertvector(f, bf16x2));
}
__device__ __forceinline__ f32x2 fma2(f32x2 a, f32x2 b, f32x2 c) { return __builtin_elementwise_fma(a, b, c); }
template <int CTRL>
__device__ __forceinline__ float dpp_add(float v) {
    return v + __int_as_float(__builtin_amdgcn_update_dpp(0, __float_as_int(v), CTRL, 0xf, 0xf, true));
}
__device__ __forceinline__ void swap32(float& a, float& b) {
    auto r = __builtin_amdgcn_permlane32_swap(__float_as_uint(a), __float_as_uint(b), false, false);
    a = __uint_as_float(r[0]); b = __uint_as_float(r[1]);
}
__device__ __forceinline__ void swap16(float& a, float& b) {
    auto r = __builtin_amdgcn_permlane16_swap(__float_as_uint(a), __float_as_uint(b), false, false);
    a = __uint_as_float(r[0]); b = __uint_as_float(r[1]);
}
#define SB() __builtin_amdgcn_sched_barrier(0)

__global__ __launch_bounds__(64 * WPB) void grn_kernel(const void* __restrict__ x, const void* __restrict__ g,
                                                       const void* __restrict__ b, void* __restrict__ out) {
    unsigned long long r0 = __builtin_amdgcn_s_memrealtime();
    __shared__ u32x4 gb[WPB * 64];
    SB();
    const int lane = threadIdx.x & 63;
    const int wv = __builtin_amdgcn_readfirstlane(threadIdx.x >> 6);
    const int batch = (blockIdx.x & 7) * (NBLK / 8) + (blockIdx.x >> 3) + wv * NBLK;
    const int so = batch * BATCH;
    const int vm = lane * 16;
    int vt = (lane >> 4) * ROW + 1024 + (lane & 15) * 16, so4 = so + 4 * ROW, vp = vm + (wv >> 1) * 1024;
    asm volatile("" : "+v"(vt), "+s"(so4), "+v"(vp) : "v"(vm), "s"(so));
    SB();
    const __amdgpu_buffer_rsrc_t rx = __builtin_amdgcn_make_buffer_rsrc((void*)x, 0, B * BATCH, 0x00020000);
    const __amdgpu_buffer_rsrc_t ro = __builtin_amdgcn_make_buffer_rsrc(out, 0, B * BATCH, 0x00020000);
    SB();
    u32x4 t[2], a[8];
    t[0] = __builtin_amdgcn_raw_buffer_load_b128(rx, vt, so, LD_AUX);
    t[1] = __builtin_amdgcn_raw_buffer_load_b128(rx, vt, so4, LD_AUX);
    SB();
    const __amdgpu_buffer_rsrc_t rp = __builtin_amdgcn_make_buffer_rsrc((void*)((wv & 1) ? b : g), 0, ROW, 0x00020000);
    const u32x4 pc = __builtin_amdgcn_raw_buffer_load_b128(rp, vp, 0, 0);
    SB();
#pragma unroll
    for (int s = 0; s < 8; ++s) {
        a[s] = __builtin_amdgcn_raw_buffer_load_b128(rx, vm + (s & 3) * ROW, s < 4 ? so : so4, LD_AUX);
        SB();
        if (s == SPLIT) {
            asm volatile("v_nop");
            SB();
        }
    }

    f32x2 xt[2][4], st[4];
#pragma unroll
    for (int j = 0; j < 4; ++j) { xt[0][j] = up(t[0][j]); st[j] = xt[0][j] * xt[0][j]; }
#pragma unroll
    for (int j = 0; j < 4; ++j) { xt[1][j] = up(t[1][j]); st[j] = fma2(xt[1][j], xt[1][j], st[j]); }
    float u0 = st[0][0], u1 = st[0][1], u2 = st[1][0], u3 = st[1][1];
    float u4 = st[2][0], u5 = st[2][1], u6 = st[3][0], u7 = st[3][1];
    swap32(u0, u4); swap32(u1, u5); swap32(u2, u6); swap32(u3, u7);
    u0 += u4; u1 += u5; u2 += u6; u3 += u7;
    swap16(u0, u2); swap16(u1, u3);
    u0 += u2; u1 += u3;
    u0 = __builtin_amdgcn_sqrtf(u0); u1 = __builtin_amdgcn_sqrtf(u1);
    float p = u0 + u1;
    u2 = u0; u3 = u1;
    swap16(u0, u2); swap16(u1, u3);
    u4 = u0; u5 = u1; u6 = u2; u7 = u3;
    swap32(u0, u4); swap32(u1, u5); swap32(u2, u6); swap32(u3, u7);
    f32x2 nt[4] = {{u0, u1}, {u2, u3}, {u4, u5}, {u6, u7}};
    SB();
    gb[threadIdx.x] = pc;
    asm volatile("s_waitcnt lgkmcnt(0)\n\ts_barrier" ::: "memory");
    const u32x4 ga = gb[lane], ba = gb[64 + lane], gt = gb[128 + (lane & 15)], bt = gb[192 + (lane & 15)];
    SB();

    f32x2 xa[8][4], sa[4], fg[4], fa[4], ft[4];
#pragma unroll
    for (int s = 0; s < 8; ++s) {
#pragma unroll
        for (int j = 0; j < 4; ++j) {
            xa[s][j] = up(a[s][j]);
            sa[j] = s == 0 ? xa[s][j] * xa[s][j] : fma2(xa[s][j], xa[s][j], sa[j]);
        }
        asm volatile("" : "+v"(sa[0]), "+v"(sa[1]), "+v"(sa[2]), "+v"(sa[3]));
        if (s == SPLIT) {
#pragma unroll
            for (int j = 0; j < 4; ++j) { fg[j] = up(ga[j]); nt[j] *= up(gt[j]); fa[j] = up(ba[j]); ft[j] = up(bt[j]); }
            asm volatile("" : "+v"(fg[0]), "+v"(fg[1]), "+v"(fg[2]), "+v"(fg[3]), "+v"(nt[0]), "+v"(nt[1]), "+v"(nt[2]), "+v"(nt[3]));
            asm volatile("" : "+v"(fa[0]), "+v"(fa[1]), "+v"(fa[2]), "+v"(fa[3]), "+v"(ft[0]), "+v"(ft[1]), "+v"(ft[2]), "+v"(ft[3]));
        }
        SB();
    }
#pragma unroll
    for (int j = 0; j < 4; ++j) {
        sa[j][0] = __builtin_amdgcn_sqrtf(sa[j][0]);
        sa[j][1] = __builtin_amdgcn_sqrtf(sa[j][1]);
    }
    asm volatile("" :: "v"(sa[0]), "v"(sa[3]));
    unsigned long long r3 = __builtin_amdgcn_s_memrealtime();
    SB();
    f32x2 q = (sa[0] + sa[1]) + (sa[2] + sa[3]);
    p += q[0] + q[1];
    p = dpp_add<0xB1>(p);
    p = dpp_add<0x4E>(p);
    p = dpp_add<0x141>(p);
    p = dpp_add<0x140>(p);
    { float r = p; swap16(p, r); p += r; r = p; swap32(p, r); p += r; }
    const float inv = __builtin_amdgcn_rcpf(p * (1.0f / D) + 1e-6f);
    const f32x2 inv2 = {inv, inv}, one = {1.0f, 1.0f};
    f32x2 ca[4], ct[4];
#pragma unroll
    for (int j = 0; j < 4; ++j) {
        ca[j] = fma2(fg[j] * sa[j], inv2, one);
        ct[j] = fma2(nt[j], inv2, one);
    }
    SB();
    u32x4 o[10];
#pragma unroll
    for (int s = 0; s < 10; ++s) {
#pragma unroll
        for (int j = 0; j < 4; ++j)
            o[s][j] = s < 8 ? pk(fma2(xa[s & 7][j], ca[j], fa[j])) : pk(fma2(xt[s & 1][j], ct[j], ft[j]));
        if (s > 0) asm volatile("" :: "v"(o[s - 1]), "v"(o[s]));
        if (s < 8) __builtin_amdgcn_raw_buffer_store_b128(o[s], ro, vm + (s & 3) * ROW, s < 4 ? so : so4, ST_AUX);
        else __builtin_amdgcn_raw_buffer_store_b128(o[s], ro, vt, (s & 1) ? so4 : so, ST_AUX);
        SB();
    }
    {
        unsigned long long r6 = __builtin_amdgcn_s_memrealtime();
        unsigned long long v = r3 - r0;
        if (v > 600) v = 600;
        for (int it = 0; it < 200000 && __builtin_amdgcn_s_memrealtime() - r6 < v * 50; ++it) __builtin_amdgcn_s_sleep(4);
    }
}

extern "C" void kernel_launch(void* const* d_in, const int* in_sizes, int n_in,
                              void* d_out, int out_size, void* d_ws, size_t ws_size,
                              hipStream_t stream) {
    grn_kernel<<<NBLK, 64 * WPB, 0, stream>>>(d_in[0], d_in[1], d_in[2], d_out);
}
```

```cpp
#include <hip/hip_runtime.h>
#include <hip/hip_bf16.h>
#include <stdint.h>

typedef uint32_t u32x4 __attribute__((ext_vector_type(4)));
typedef float f32x2 __attribute__((ext_vector_type(2)));
typedef __bf16 bf16x2 __attribute__((ext_vector_type(2)));

constexpr int B = 1024, S = 8, D = 640;
constexpr int WPB = 4;
constexpr int NBLK = B / WPB;
constexpr int ROW = D * 2, BATCH = S * ROW;
constexpr int SPLIT = 4;
constexpr int LD_AUX = 2;
constexpr int ST_AUX = 19;

__device__ __forceinline__ f32x2 up(uint32_t v) {
    f32x2 r = {__uint_as_float(v << 16), __uint_as_float(v & 0xffff0000u)};
    return r;
}
__device__ __forceinline__ uint32_t pk(f32x2 f) {
    return __builtin_bit_cast(uint32_t, __builtin_convertvector(f, bf16x2));
}
__device__ __forceinline__ f32x2 fma2(f32x2 a, f32x2 b, f32x2 c) { return __builtin_elementwise_fma(a, b, c); }
template <int CTRL>
__device__ __forceinline__ float dpp_add(float v) {
    return v + __int_as_float(__builtin_amdgcn_update_dpp(0, __float_as_int(v), CTRL, 0xf, 0xf, true));
}
__device__ __forceinline__ void swap32(float& a, float& b) {
    auto r = __builtin_amdgcn_permlane32_swap(__float_as_uint(a), __float_as_uint(b), false, false);
    a = __uint_as_float(r[0]); b = __uint_as_float(r[1]);
}
__device__ __forceinline__ void swap16(float& a, float& b) {
    auto r = __builtin_amdgcn_permlane16_swap(__float_as_uint(a), __float_as_uint(b), false, false);
    a = __uint_as_float(r[0]); b = __uint_as_float(r[1]);
}
#define SB() __builtin_amdgcn_sched_barrier(0)

__global__ __launch_bounds__(64 * WPB) void grn_kernel(const void* __restrict__ x, const void* __restrict__ g,
                                                       const void* __restrict__ b, void* __restrict__ out) {
    __shared__ u32x4 gb[WPB * 64];
    SB();
    const int lane = threadIdx.x & 63;
    const int wv = __builtin_amdgcn_readfirstlane(threadIdx.x >> 6);
    const int batch = (blockIdx.x & 7) * (NBLK / 8) + (blockIdx.x >> 3) + wv * NBLK;
    const int so = batch * BATCH;
    const int vm = lane * 16;
    int vt = (lane >> 4) * ROW + 1024 + (lane & 15) * 16, so4 = so + 4 * ROW, vp = vm + (wv >> 1) * 1024;
    asm volatile("" : "+v"(vt), "+s"(so4), "+v"(vp) : "v"(vm), "s"(so));
    SB();
    const __amdgpu_buffer_rsrc_t rx = __builtin_amdgcn_make_buffer_rsrc((void*)x, 0, B * BATCH, 0x00020000);
    const __amdgpu_buffer_rsrc_t ro = __builtin_amdgcn_make_buffer_rsrc(out, 0, B * BATCH, 0x00020000);
    SB();
    u32x4 t[2], a[8];
    t[0] = __builtin_amdgcn_raw_buffer_load_b128(rx, vt, so, LD_AUX);
    t[1] = __builtin_amdgcn_raw_buffer_load_b128(rx, vt, so4, LD_AUX);
    SB();
    const __amdgpu_buffer_rsrc_t rp = __builtin_amdgcn_make_buffer_rsrc((void*)((wv & 1) ? b : g), 0, ROW, 0x00020000);
    const u32x4 pc = __builtin_amdgcn_raw_buffer_load_b128(rp, vp, 0, 0);
    SB();
#pragma unroll
    for (int s = 0; s < 8; ++s) {
        a[s] = __builtin_amdgcn_raw_buffer_load_b128(rx, vm + (s & 3) * ROW, s < 4 ? so : so4, LD_AUX);
        SB();
        if (s == SPLIT) {
            asm volatile("v_nop");
            SB();
        }
    }

    f32x2 xt[2][4], st[4];
#pragma unroll
    for (int j = 0; j < 4; ++j) { xt[0][j] = up(t[0][j]); st[j] = xt[0][j] * xt[0][j]; }
#pragma unroll
    for (int j = 0; j < 4; ++j) { xt[1][j] = up(t[1][j]); st[j] = fma2(xt[1][j], xt[1][j], st[j]); }
    float u0 = st[0][0], u1 = st[0][1], u2 = st[1][0], u3 = st[1][1];
    float u4 = st[2][0], u5 = st[2][1], u6 = st[3][0], u7 = st[3][1];
    swap32(u0, u4); swap32(u1, u5); swap32(u2, u6); swap32(u3, u7);
    u0 += u4; u1 += u5; u2 += u6; u3 += u7;
    swap16(u0, u2); swap16(u1, u3);
    u0 += u2; u1 += u3;
    u0 = __builtin_amdgcn_sqrtf(u0); u1 = __builtin_amdgcn_sqrtf(u1);
    float p = u0 + u1;
    u2 = u0; u3 = u1;
    swap16(u0, u2); swap16(u1, u3);
    u4 = u0; u5 = u1; u6 = u2; u7 = u3;
    swap32(u0, u4); swap32(u1, u5); swap32(u2, u6); swap32(u3, u7);
    f32x2 nt[4] = {{u0, u1}, {u2, u3}, {u4, u5}, {u6, u7}};
    SB();
    gb[threadIdx.x] = pc;
    asm volatile("s_waitcnt lgkmcnt(0)\n\ts_barrier" ::: "memory");
    const u32x4 ga = gb[lane], ba = gb[64 + lane], gt = gb[128 + (lane & 15)], bt = gb[192 + (lane & 15)];
    SB();

    f32x2 xa[8][4], sa[4], fg[4], fa[4], ft[4];
#pragma unroll
    for (int s = 0; s < 8; ++s) {
#pragma unroll
        for (int j = 0; j < 4; ++j) {
            xa[s][j] = up(a[s][j]);
            sa[j] = s == 0 ? xa[s][j] * xa[s][j] : fma2(xa[s][j], xa[s][j], sa[j]);
        }
        asm volatile("" : "+v"(sa[0]), "+v"(sa[1]), "+v"(sa[2]), "+v"(sa[3]));
        if (s == SPLIT) {
#pragma unroll
            for (int j = 0; j < 4; ++j) { fg[j] = up(ga[j]); nt[j] *= up(gt[j]); fa[j] = up(ba[j]); ft[j] = up(bt[j]); }
            asm volatile("" : "+v"(fg[0]), "+v"(fg[1]), "+v"(fg[2]), "+v"(fg[3]), "+v"(nt[0]), "+v"(nt[1]), "+v"(nt[2]), "+v"(nt[3]));
            asm volatile("" : "+v"(fa[0]), "+v"(fa[1]), "+v"(fa[2]), "+v"(fa[3]), "+v"(ft[0]), "+v"(ft[1]), "+v"(ft[2]), "+v"(ft[3]));
        }
        SB();
    }
#pragma unroll
    for (int j = 0; j < 4; ++j) {
        sa[j][0] = __builtin_amdgcn_sqrtf(sa[j][0]);
        sa[j][1] = __builtin_amdgcn_sqrtf(sa[j][1]);
    }
    f32x2 q = (sa[0] + sa[1]) + (sa[2] + sa[3]);
    p += q[0] + q[1];
    p = dpp_add<0xB1>(p);
    p = dpp_add<0x4E>(p);
    p = dpp_add<0x141>(p);
    p = dpp_add<0x140>(p);
    { float r = p; swap16(p, r); p += r; r = p; swap32(p, r); p += r; }
    const float inv = __builtin_amdgcn_rcpf(p * (1.0f / D) + 1e-6f);
    const f32x2 inv2 = {inv, inv}, one = {1.0f, 1.0f};
    f32x2 ca[4], ct[4];
#pragma unroll
    for (int j = 0; j < 4; ++j) {
        ca[j] = fma2(fg[j] * sa[j], inv2, one);
        ct[j] = fma2(nt[j], inv2, one);
    }
    SB();
    u32x4 o[10];
#pragma unroll
    for (int s = 0; s < 10; ++s) {
#pragma unroll
        for (int j = 0; j < 4; ++j)
            o[s][j] = s < 8 ? pk(fma2(xa[s & 7][j], ca[j], fa[j])) : pk(fma2(xt[s & 1][j], ct[j], ft[j]));
        if (s > 0) asm volatile("" :: "v"(o[s - 1]), "v"(o[s]));
        if (s < 8) __builtin_amdgcn_raw_buffer_store_b128(o[s], ro, vm + (s & 3) * ROW, s < 4 ? so : so4, ST_AUX);
        else __builtin_amdgcn_raw_buffer_store_b128(o[s], ro, vt, (s & 1) ? so4 : so, ST_AUX);
        SB();
    }
    {
        unsigned long long t0, t1;
        asm volatile("s_waitcnt vmcnt(0)\n s_memrealtime %0\n s_waitcnt lgkmcnt(0)\n s_branch .Lfar\n .Lback:\n s_memrealtime %1\n s_waitcnt lgkmcnt(0)\n s_branch .Lon\n"
                     ".rept 1024\n s_nop 0\n .endr\n .Lfar: s_branch .Lback\n .rept 256\n s_nop 0\n .endr\n .Lon:" : "=&s"(t0), "=&s"(t1));
        unsigned long long r6 = __builtin_amdgcn_s_memrealtime();
        unsigned long long v = t1 - t0;
        if (v > 300) v = 300;
        for (int it = 0; it < 200000 && __builtin_amdgcn_s_memrealtime() - r6 < v * 50; ++it) __builtin_amdgcn_s_sleep(4);
    }
}

extern "C" void kernel_launch(void* const* d_in, const int* in_sizes, int n_in,
                              void* d_out, int out_size, void* d_ws, size_t ws_size,
                              hipStream_t stream) {
    grn_kernel<<<NBLK, 64 * WPB, 0, stream>>>(d_in[0], d_in[1], d_in[2], d_out);
}
```

```cpp
#include <hip/hip_runtime.h>
#include <hip/hip_bf16.h>
#include <stdint.h>

typedef uint32_t u32x4 __attribute__((ext_vector_type(4)));
typedef float f32x2 __attribute__((ext_vector_type(2)));
typedef __bf16 bf16x2 __attribute__((ext_vector_type(2)));

constexpr int B = 1024, S = 8, D = 640;
constexpr int WPB = 4;
constexpr int NBLK = B / WPB;
constexpr int ROW = D * 2, BATCH = S * ROW;
constexpr int SPLIT = 4;
constexpr int LD_AUX = 2;
constexpr int ST_AUX = 19;

__device__ __forceinline__ f32x2 up(uint32_t v) {
    f32x2 r = {__uint_as_float(v << 16), __uint_as_float(v & 0xffff0000u)};
    return r;
}
__device__ __forceinline__ uint32_t pk(f32x2 f) {
    return __builtin_bit_cast(uint32_t, __builtin_convertvector(f, bf16x2));
}
__device__ __forceinline__ f32x2 fma2(f32x2 a, f32x2 b, f32x2 c) { return __builtin_elementwise_fma(a, b, c); }
template <int CTRL>
__device__ __forceinline__ float dpp_add(float v) {
    return v + __int_as_float(__builtin_amdgcn_update_dpp(0, __float_as_int(v), CTRL, 0xf, 0xf, true));
}
__device__ __forceinline__ void swap32(float& a, float& b) {
    auto r = __builtin_amdgcn_permlane32_swap(__float_as_uint(a), __float_as_uint(b), false, false);
    a = __uint_as_float(r[0]); b = __uint_as_float(r[1]);
}
__device__ __forceinline__ void swap16(float& a, float& b) {
    auto r = __builtin_amdgcn_permlane16_swap(__float_as_uint(a), __float_as_uint(b), false, false);
    a = __uint_as_float(r[0]); b = __uint_as_float(r[1]);
}
#define SB() __builtin_amdgcn_sched_barrier(0)

__global__ __launch_bounds__(64 * WPB) __attribute__((aligned(4096)))
void grn_kernel(const void* __restrict__ x, const void* __restrict__ g, const void* __restrict__ b,
                void* __restrict__ out) {
    __shared__ u32x4 gb[WPB * 64];
    SB();
    const int lane = threadIdx.x & 63;
    const int wv = __builtin_amdgcn_readfirstlane(threadIdx.x >> 6);
    uint32_t ahead = 0;
    if (wv == 0) {
        unsigned long long here;
        asm volatile("s_getpc_b64 %0" : "=s"(here));
        asm volatile("global_load_dword %0, %1, %2 offset:896" : "=v"(ahead) : "v"((lane & 15) * 128), "s"(here));
    }
    SB();
    const int batch = (blockIdx.x & 7) * (NBLK / 8) + (blockIdx.x >> 3) + wv * NBLK;
    const int so = batch * BATCH;
    const int vm = lane * 16;
    int vt = (lane >> 4) * ROW + 1024 + (lane & 15) * 16, so4 = so + 4 * ROW, vp = vm + (wv >> 1) * 1024;
    asm volatile("" : "+v"(vt), "+s"(so4), "+v"(vp) : "v"(vm), "s"(so));
    SB();
    const __amdgpu_buffer_rsrc_t rx = __builtin_amdgcn_make_buffer_rsrc((void*)x, 0, B * BATCH, 0x00020000);
    const __amdgpu_buffer_rsrc_t ro = __builtin_amdgcn_make_buffer_rsrc(out, 0, B * BATCH, 0x00020000);
    SB();
    u32x4 t[2], a[8];
    t[0] = __builtin_amdgcn_raw_buffer_load_b128(rx, vt, so, LD_AUX);
    t[1] = __builtin_amdgcn_raw_buffer_load_b128(rx, vt, so4, LD_AUX);
    SB();
    const __amdgpu_buffer_rsrc_t rp = __builtin_amdgcn_make_buffer_rsrc((void*)((wv & 1) ? b : g), 0, ROW, 0x00020000);
    const u32x4 pc = __builtin_amdgcn_raw_buffer_load_b128(rp, vp, 0, 0);
    SB();
#pragma unroll
    for (int s = 0; s < 8; ++s) {
        a[s] = __builtin_amdgcn_raw_buffer_load_b128(rx, vm + (s & 3) * ROW, s < 4 ? so : so4, LD_AUX);
        SB();
        if (s == SPLIT) {
            asm volatile("v_nop");
            SB();
        }
    }

    f32x2 xt[2][4], st[4];
#pragma unroll
    for (int j = 0; j < 4; ++j) { xt[0][j] = up(t[0][j]); st[j] = xt[0][j] * xt[0][j]; }
#pragma unroll
    for (int j = 0; j < 4; ++j) { xt[1][j] = up(t[1][j]); st[j] = fma2(xt[1][j], xt[1][j], st[j]); }
    asm volatile("" :: "v"(ahead), "v"(st[0]));
    float u0 = st[0][0], u1 = st[0][1], u2 = st[1][0], u3 = st[1][1];
    float u4 = st[2][0], u5 = st[2][1], u6 = st[3][0], u7 = st[3][1];
    swap32(u0, u4); swap32(u1, u5); swap32(u2, u6); swap32(u3, u7);
    u0 += u4; u1 += u5; u2 += u6; u3 += u7;
    swap16(u0, u2); swap16(u1, u3);
    u0 += u2; u1 += u3;
    u0 = __builtin_amdgcn_sqrtf(u0); u1 = __builtin_amdgcn_sqrtf(u1);
    float p = u0 + u1;
    u2 = u0; u3 = u1;
    swap16(u0, u2); swap16(u1, u3);
    u4 = u0; u5 = u1; u6 = u2; u7 = u3;
    swap32(u0, u4); swap32(u1, u5); swap32(u2, u6); swap32(u3, u7);
    f32x2 nt[4] = {{u0, u1}, {u2, u3}, {u4, u5}, {u6, u7}};
    SB();
    gb[threadIdx.x] = pc;
    asm volatile("s_waitcnt lgkmcnt(0)\n\ts_barrier" ::: "memory");
    const u32x4 ga = gb[lane], ba = gb[64 + lane], gt = gb[128 + (lane & 15)], bt = gb[192 + (lane & 15)];
    SB();

    f32x2 xa[8][4], sa[4], fg[4], fa[4], ft[4];
#pragma unroll
    for (int s = 0; s < 8; ++s) {
#pragma unroll
        for (int j = 0; j < 4; ++j) {
            xa[s][j] = up(a[s][j]);
            sa[j] = s == 0 ? xa[s][j] * xa[s][j] : fma2(xa[s][j], xa[s][j], sa[j]);
        }
        asm volatile("" : "+v"(sa[0]), "+v"(sa[1]), "+v"(sa[2]), "+v"(sa[3]));
        if (s == SPLIT) {
#pragma unroll
            for (int j = 0; j < 4; ++j) { fg[j] = up(ga[j]); nt[j] *= up(gt[j]); fa[j] = up(ba[j]); ft[j] = up(bt[j]); }
            asm volatile("" : "+v"(fg[0]), "+v"(fg[1]), "+v"(fg[2]), "+v"(fg[3]), "+v"(nt[0]), "+v"(nt[1]), "+v"(nt[2]), "+v"(nt[3]));
            asm volatile("" : "+v"(fa[0]), "+v"(fa[1]), "+v"(fa[2]), "+v"(fa[3]), "+v"(ft[0]), "+v"(ft[1]), "+v"(ft[2]), "+v"(ft[3]));
        }
        SB();
    }
#pragma unroll
    for (int j = 0; j < 4; ++j) {
        sa[j][0] = __builtin_amdgcn_sqrtf(sa[j][0]);
        sa[j][1] = __builtin_amdgcn_sqrtf(sa[j][1]);
    }
    f32x2 q = (sa[0] + sa[1]) + (sa[2] + sa[3]);
    p += q[0] + q[1];
    p = dpp_add<0xB1>(p);
    p = dpp_add<0x4E>(p);
    p = dpp_add<0x141>(p);
    p = dpp_add<0x140>(p);
    { float r = p; swap16(p, r); p += r; r = p; swap32(p, r); p += r; }
    const float inv = __builtin_amdgcn_rcpf(p * (1.0f / D) + 1e-6f);
    const f32x2 inv2 = {inv, inv}, one = {1.0f, 1.0f};
    f32x2 ca[4], ct[4];
#pragma unroll
    for (int j = 0; j < 4; ++j) {
        ca[j] = fma2(fg[j] * sa[j], inv2, one);
        ct[j] = fma2(nt[j], inv2, one);
    }
    SB();
    u32x4 o[10];
#pragma unroll
    for (int s = 0; s < 10; ++s) {
#pragma unroll
        for (int j = 0; j < 4; ++j)
            o[s][j] = s < 8 ? pk(fma2(xa[s & 7][j], ca[j], fa[j])) : pk(fma2(xt[s & 1][j], ct[j], ft[j]));
        if (s > 0) asm volatile("" :: "v"(o[s - 1]), "v"(o[s]));
        if (s < 8) __builtin_amdgcn_raw_buffer_store_b128(o[s], ro, vm + (s & 3) * ROW, s < 4 ? so : so4, ST_AUX);
        else __builtin_amdgcn_raw_buffer_store_b128(o[s], ro, vt, (s & 1) ? so4 : so, ST_AUX);
        SB();
    }
}

extern "C" void kernel_launch(void* const* d_in, const int* in_sizes, int n_in,
                              void* d_out, int out_size, void* d_ws, size_t ws_size,
                              hipStream_t stream) {
    grn_kernel<<<NBLK, 64 * WPB, 0, stream>>>(d_in[0], d_in[1], d_in[2], d_out);
}
```
